```python
import torch

def _softmax(x, axis=-1):
    return torch.softmax(x, dim=axis)
def _gelu(x):
    return torch.nn.functional.gelu(x, approximate="tanh")
def _silu(x):
    return torch.nn.functional.silu(x)
def _sigmoid(x):
    return torch.sigmoid(x)
def _layernorm(x, axis=-1, eps=1e-5):
    m = x.mean(dim=axis, keepdim=True)
    v = x.var(dim=axis, keepdim=True, unbiased=False)
    return (x - m) / torch.sqrt(v + eps)
def _scatter_add(tgt, idx, src, axis=0):
    return tgt.clone().index_add_(axis, idx, src)
def _segment_sum(src, seg, n):
    out = torch.zeros((n,) + src.shape[1:], dtype=src.dtype)
    return out.index_add_(0, seg, src)
def _pad(x, pw):
    flat = tuple(v for pair in reversed(pw) for v in pair)
    return torch.nn.functional.pad(x, flat)

kernel_name = 'interleave_layers__spectral_fnet__dense_activation_squared_relu__prompt'

def setup_inputs():

    _w = (lambda a: (lambda t: t.to(torch.bfloat16) if t.is_floating_point() else t)(torch.as_tensor(a)))
    def _A_setup():
        B, S, D = 1, 2048, 512
        ns = torch.arange(S)[:, None] * torch.arange(S)[None, :]
        nd = torch.arange(D)[:, None] * torch.arange(D)[None, :]
        Ws_re = torch.cos(-2.0 * torch.pi * ns / S).to(float)
        Ws_im = torch.sin(-2.0 * torch.pi * ns / S).to(float)
        Wd_re = torch.cos(-2.0 * torch.pi * nd / D).to(float)
        Wd_im = torch.sin(-2.0 * torch.pi * nd / D).to(float)
        return {
            "x": _w((lambda *s: torch.randn(*s, dtype=torch.bfloat16) * 0.05)(B, S, D)),
            "dft_seq_re": _w(Ws_re),
            "dft_seq_im": _w(Ws_im),
            "dft_hid_re": _w(Wd_re),
            "dft_hid_im": _w(Wd_im),
        }
    _A_in = _A_setup()

    def _B_setup():
        B, S, D, F = 1, 2048, 512, 1792
        return {
            "x": _w((lambda *s: torch.randn(*s, dtype=torch.bfloat16) * 0.05)(B, S, D)),
            "w1": _w((lambda *s: torch.randn(*s, dtype=torch.bfloat16) * 0.4)(D, F)),
            "b1": _w((lambda *s: torch.randn(*s, dtype=torch.bfloat16) * 0.4)(F)),
            "w2": _w((lambda *s: torch.randn(*s, dtype=torch.bfloat16) * 0.4)(F, D)),
            "b2": _w((lambda *s: torch.randn(*s, dtype=torch.bfloat16) * 0.4)(D)),
        }
    _B_in = _B_setup()

    return {
        "A_x": _A_in["x"],
        "A_dft_seq_re": _A_in["dft_seq_re"],
        "A_dft_seq_im": _A_in["dft_seq_im"],
        "A_dft_hid_re": _A_in["dft_hid_re"],
        "A_dft_hid_im": _A_in["dft_hid_im"],
        "B_x": _B_in["x"],
        "B_w1": _B_in["w1"],
        "B_b1": _B_in["b1"],
        "B_w2": _B_in["w2"],
        "B_b2": _B_in["b2"],
    }

def reference(A_x, A_dft_seq_re, A_dft_seq_im, A_dft_hid_re, A_dft_hid_im, B_x, B_w1, B_b1, B_w2, B_b2):
    def _A_ref(x, dft_seq_re, dft_seq_im, dft_hid_re, dft_hid_im):
        a = torch.einsum("bsd,ed->bse", x, dft_hid_re)
        b = torch.einsum("bsd,ed->bse", x, dft_hid_im)
        y = torch.einsum("ts,bse->bte", dft_seq_re, a) - torch.einsum("ts,bse->bte", dft_seq_im, b)
        return y

    def _B_ref(x, w1, b1, w2, b2):
        h = torch.einsum("bsd,df->bsf", x, w1) + b1
        zero = torch.zeros_like(h)
        r = torch.where(h > zero, h, zero)
        h = r * r
        out = torch.einsum("bsf,fd->bsd", h, w2) + b2
        return out

    x = A_x
    x = x + _A_ref(_layernorm(x, axis=-1), A_dft_seq_re, A_dft_seq_im, A_dft_hid_re, A_dft_hid_im)
    x = x + _B_ref(_layernorm(x, axis=-1), B_w1, B_b1, B_w2, B_b2)
    x = x + _A_ref(_layernorm(x, axis=-1), A_dft_seq_re, A_dft_seq_im, A_dft_hid_re, A_dft_hid_im)
    x = x + _B_ref(_layernorm(x, axis=-1), B_w1, B_b1, B_w2, B_b2)
    return x
```

```cpp
#include <hip/hip_runtime.h>
#include <hip/hip_bf16.h>
#include <stdint.h>

typedef unsigned short u16;
using bf16x8 = __attribute__((ext_vector_type(8))) short;
using f32x4 = __attribute__((ext_vector_type(4))) float;

constexpr int S_ = 2048, D_ = 512, F_ = 1792;

__device__ __forceinline__ float bf2f(u16 u) { return __uint_as_float(((unsigned)u) << 16); }
__device__ __forceinline__ u16 f2bf(float f) {
    __bf16 h = (__bf16)f;
    return __builtin_bit_cast(u16, h);
}
__device__ __forceinline__ float rbf(float f) { return bf2f(f2bf(f)); }

template <int NPART>
__global__ __launch_bounds__(256) void row_kernel(const u16* __restrict__ x_old, const float* __restrict__ partial,
                                                  const u16* __restrict__ bias, u16* __restrict__ x_new,
                                                  u16* __restrict__ xn) {
    int row = blockIdx.x * 4 + (threadIdx.x >> 6);
    int lane = threadIdx.x & 63;
    float v[8];
    {
        uint4 raw = *(const uint4*)(x_old + (long)row * D_ + lane * 8);
        unsigned w[4] = {raw.x, raw.y, raw.z, raw.w};
#pragma unroll
        for (int i = 0; i < 4; ++i) {
            v[2 * i] = __uint_as_float(w[i] << 16);
            v[2 * i + 1] = __uint_as_float(w[i] & 0xffff0000u);
        }
    }
    if (NPART > 0) {
        float acc[8] = {0, 0, 0, 0, 0, 0, 0, 0};
#pragma unroll
        for (int p = 0; p < NPART; ++p) {
            const float* src = partial + (long)p * S_ * D_ + (long)row * D_ + lane * 8;
            float4 a = *(const float4*)src;
            float4 b = *(const float4*)(src + 4);
            acc[0] += a.x; acc[1] += a.y; acc[2] += a.z; acc[3] += a.w;
            acc[4] += b.x; acc[5] += b.y; acc[6] += b.z; acc[7] += b.w;
        }
#pragma unroll
        for (int i = 0; i < 8; ++i) {
            float y = rbf(acc[i]);
            if (bias) y = rbf(y + bf2f(bias[lane * 8 + i]));
            v[i] = rbf(v[i] + y);
        }
        uint4 o;
        o.x = f2bf(v[0]) | ((unsigned)f2bf(v[1]) << 16);
        o.y = f2bf(v[2]) | ((unsigned)f2bf(v[3]) << 16);
        o.z = f2bf(v[4]) | ((unsigned)f2bf(v[5]) << 16);
        o.w = f2bf(v[6]) | ((unsigned)f2bf(v[7]) << 16);
        *(uint4*)(x_new + (long)row * D_ + lane * 8) = o;
    }
    if (xn == nullptr) return;
    float s = 0;
#pragma unroll
    for (int i = 0; i < 8; ++i) s += v[i];
#pragma unroll
    for (int off = 32; off > 0; off >>= 1) s += __shfl_xor(s, off);
    float mean = s * (1.0f / D_);
    float q = 0;
#pragma unroll
    for (int i = 0; i < 8; ++i) { float d = v[i] - mean; q += d * d; }
#pragma unroll
    for (int off = 32; off > 0; off >>= 1) q += __shfl_xor(q, off);
    float var = rbf(q * (1.0f / D_));
    float mb = rbf(mean);
    float den = rbf(sqrtf(rbf(var + 1e-5f)));
    u16 o[8];
#pragma unroll
    for (int i = 0; i < 8; ++i) {
        float d = rbf(v[i] - mb);
        o[i] = f2bf(d / den);
    }
    uint4 ov;
    ov.x = o[0] | ((unsigned)o[1] << 16);
    ov.y = o[2] | ((unsigned)o[3] << 16);
    ov.z = o[4] | ((unsigned)o[5] << 16);
    ov.w = o[6] | ((unsigned)o[7] << 16);
    *(uint4*)(xn + (long)row * D_ + lane * 8) = ov;
}

__global__ __launch_bounds__(256) void transpose_kernel(const u16* __restrict__ src, u16* __restrict__ dst, int R, int C) {
    __shared__ u16 tile[64][66];
    int bx = blockIdx.x % (C / 64), by = blockIdx.x / (C / 64);
    int tx = threadIdx.x & 63, ty = threadIdx.x >> 6;
    for (int i = ty; i < 64; i += 4) tile[i][tx] = src[(long)(by * 64 + i) * C + bx * 64 + tx];
    __syncthreads();
    for (int i = ty; i < 64; i += 4) dst[(long)(bx * 64 + i) * R + by * 64 + tx] = tile[tx][i];
}

constexpr int BM = 128, BN = 128, BK = 64;
constexpr int TILE_BYTES = 128 * 128;
constexpr int STAGE_BYTES = 2 * TILE_BYTES;
constexpr int NSTAGE = 3;
constexpr int GEMM_LDS = NSTAGE * STAGE_BYTES;

enum { EPI_ABT = 0, EPI_PARTIAL = 1, EPI_RELU2 = 2 };

__device__ __forceinline__ void stage_tile(const u16* g, int ld, char* lds_tile, int wave, int lane) {
#pragma unroll
    for (int i = 0; i < 4; ++i) {
        int p = (i * 4 + wave) * 1024 + lane * 16;
        int row = p >> 7;
        int c = ((p >> 4) & 7) ^ ((row >> 1) & 7);
        __builtin_amdgcn_global_load_lds((const unsigned*)(g + (long)row * ld + c * 8),
                                         (unsigned*)(lds_tile + (i * 4 + wave) * 1024), 16, 0, 0);
    }
}

template <int EPI>
__device__ __forceinline__ void gemm_body(const u16* __restrict__ A, int lda, const u16* __restrict__ B, int ldb, int nk,
                                          void* __restrict__ Cout, int ldc, const u16* __restrict__ bias, float sign,
                                          char* smem) {
    int tid = threadIdx.x;
    int wave = tid >> 6, lane = tid & 63;
    int wr = wave >> 1, wc = wave & 1;
    int fr = lane & 15, fq = lane >> 4;

    f32x4 acc[4][4];
#pragma unroll
    for (int i = 0; i < 4; ++i)
#pragma unroll
        for (int j = 0; j < 4; ++j) acc[i][j] = f32x4{0, 0, 0, 0};

    auto stage = [&](int t, int buf) {
        stage_tile(A + t * BK, lda, smem + buf * STAGE_BYTES, wave, lane);
        stage_tile(B + t * BK, ldb, smem + buf * STAGE_BYTES + TILE_BYTES, wave, lane);
    };
    auto compute = [&](int buf) {
        const char* sa = smem + buf * STAGE_BYTES;
        const char* sb = sa + TILE_BYTES;
#pragma unroll
        for (int kk = 0; kk < 2; ++kk) {
            bf16x8 af[4], bfr[4];
#pragma unroll
            for (int i = 0; i < 4; ++i) {
                int row = wr * 64 + i * 16 + fr;
                int c = (kk * 4 + fq) ^ ((row >> 1) & 7);
                af[i] = *(const bf16x8*)(sa + row * 128 + c * 16);
            }
#pragma unroll
            for (int j = 0; j < 4; ++j) {
                int row = wc * 64 + j * 16 + fr;
                int c = (kk * 4 + fq) ^ ((row >> 1) & 7);
                bfr[j] = *(const bf16x8*)(sb + row * 128 + c * 16);
            }
#pragma unroll
            for (int i = 0; i < 4; ++i)
#pragma unroll
                for (int j = 0; j < 4; ++j)
                    acc[i][j] = __builtin_amdgcn_mfma_f32_16x16x32_bf16(bfr[j], af[i], acc[i][j], 0, 0, 0);
        }
    };

    stage(0, 0);
    if (nk > 1) stage(1, 1);
    int buf = 0;
    for (int t = 0; t < nk - 1; ++t) {
        asm volatile("s_waitcnt vmcnt(8)" ::: "memory");
        asm volatile("s_waitcnt lgkmcnt(0)" ::: "memory");
        __builtin_amdgcn_s_barrier();
        int nb = buf + 2; if (nb >= NSTAGE) nb -= NSTAGE;
        if (t + 2 < nk) stage(t + 2, nb);
        compute(buf);
        buf = buf + 1; if (buf >= NSTAGE) buf = 0;
    }
    asm volatile("s_waitcnt vmcnt(0)" ::: "memory");
    asm volatile("s_waitcnt lgkmcnt(0)" ::: "memory");
    __builtin_amdgcn_s_barrier();
    compute(buf);

#pragma unroll
    for (int i = 0; i < 4; ++i) {
        int m = wr * 64 + i * 16 + fr;
#pragma unroll
        for (int j = 0; j < 4; ++j) {
            int n = wc * 64 + j * 16 + fq * 4;
            if (EPI == EPI_PARTIAL) {
                float* C = (float*)Cout;
                *(f32x4*)(C + (long)m * ldc + n) = acc[i][j];
            } else if (EPI == EPI_ABT) {
                u16* C = (u16*)Cout;
                uint2 o;
                o.x = f2bf(sign * rbf(acc[i][j][0])) | ((unsigned)f2bf(sign * rbf(acc[i][j][1])) << 16);
                o.y = f2bf(sign * rbf(acc[i][j][2])) | ((unsigned)f2bf(sign * rbf(acc[i][j][3])) << 16);
                *(uint2*)(C + (long)m * ldc + n) = o;
            } else {
                u16* C = (u16*)Cout;
                u16 o4[4];
#pragma unroll
                for (int r = 0; r < 4; ++r) {
                    float h = rbf(rbf(acc[i][j][r]) + bf2f(bias[n + r]));
                    float rr = h > 0.f ? h : 0.f;
                    o4[r] = f2bf(rr * rr);
                }
                uint2 o;
                o.x = o4[0] | ((unsigned)o4[1] << 16);
                o.y = o4[2] | ((unsigned)o4[3] << 16);
                *(uint2*)(C + (long)m * ldc + n) = o;
            }
        }
    }
}

__global__ __launch_bounds__(256) void g1_kernel(const u16* __restrict__ Hr, const u16* __restrict__ Hi,
                                                 const u16* __restrict__ xn, u16* __restrict__ abT) {
    extern __shared__ __attribute__((aligned(16))) char smem[];
    int tm = blockIdx.x & 7, tn = blockIdx.x >> 3;
    const u16* A = (tm < 4 ? Hr : Hi) + (long)(tm & 3) * BM * D_;
    const u16* B = xn + (long)tn * BN * D_;
    float sign = tm < 4 ? 1.f : -1.f;
    gemm_body<EPI_ABT>(A, D_, B, D_, D_ / BK, abT + (long)tm * BM * S_ + tn * BN, S_, nullptr, sign, smem);
}

__global__ __launch_bounds__(256) void g2_kernel(const u16* __restrict__ Cr, const u16* __restrict__ Ci,
                                                 const u16* __restrict__ abT, float* __restrict__ partial) {
    extern __shared__ __attribute__((aligned(16))) char smem[];
    int b = blockIdx.x;
    int sk = b & 3, tn = (b >> 2) & 3, tm = b >> 4;
    const u16* A = (sk < 2 ? Cr : Ci) + (long)tm * BM * S_ + (sk & 1) * 1024;
    const u16* B = abT + (long)((sk < 2 ? 0 : 512) + tn * BN) * S_ + (sk & 1) * 1024;
    gemm_body<EPI_PARTIAL>(A, S_, B, S_, 1024 / BK, partial + (long)sk * S_ * D_ + (long)tm * BM * D_ + tn * BN, D_,
                           nullptr, 1.f, smem);
}

__global__ __launch_bounds__(256) void g3_kernel(const u16* __restrict__ xn, const u16* __restrict__ w1T,
                                                 const u16* __restrict__ b1, u16* __restrict__ h2) {
    extern __shared__ __attribute__((aligned(16))) char smem[];
    int tn = blockIdx.x % 14, tm = blockIdx.x / 14;
    gemm_body<EPI_RELU2>(xn + (long)tm * BM * D_, D_, w1T + (long)tn * BN * D_, D_, D_ / BK,
                         h2 + (long)tm * BM * F_ + tn * BN, F_, b1 + tn * BN, 1.f, smem);
}

__global__ __launch_bounds__(256) void g4_kernel(const u16* __restrict__ h2, const u16* __restrict__ w2T,
                                                 float* __restrict__ partial) {
    extern __shared__ __attribute__((aligned(16))) char smem[];
    int b = blockIdx.x;
    int sk = b & 3, tn = (b >> 2) & 3, tm = b >> 4;
    gemm_body<EPI_PARTIAL>(h2 + (long)tm * BM * F_ + sk * 448, F_, w2T + (long)tn * BN * F_ + sk * 448, F_, 448 / BK,
                           partial + (long)sk * S_ * D_ + (long)tm * BM * D_ + tn * BN, D_, nullptr, 1.f, smem);
}

extern "C" void kernel_launch(void* const* d_in, const int* in_sizes, int n_in, void* d_out, int out_size, void* d_ws,
                              size_t ws_size, hipStream_t stream) {
    const u16* x0 = (const u16*)d_in[0];
    const u16* Cr = (const u16*)d_in[1];
    const u16* Ci = (const u16*)d_in[2];
    const u16* Hr = (const u16*)d_in[3];
    const u16* Hi = (const u16*)d_in[4];
    const u16* w1 = (const u16*)d_in[6];
    const u16* b1 = (const u16*)d_in[7];
    const u16* w2 = (const u16*)d_in[8];
    const u16* b2 = (const u16*)d_in[9];
    u16* out = (u16*)d_out;

    char* ws = (char*)d_ws;
    size_t off = 0;
    auto alloc = [&](size_t bytes) { char* p = ws + off; off += (bytes + 255) & ~(size_t)255; return p; };
    u16* w1T = (u16*)alloc((size_t)F_ * D_ * 2);
    u16* w2T = (u16*)alloc((size_t)F_ * D_ * 2);
    u16* xn = (u16*)alloc((size_t)S_ * D_ * 2);
    u16* xa = (u16*)alloc((size_t)S_ * D_ * 2);
    u16* xb = (u16*)alloc((size_t)S_ * D_ * 2);
    u16* abT = (u16*)alloc((size_t)1024 * S_ * 2);
    u16* h2 = (u16*)alloc((size_t)S_ * F_ * 2);
    float* partial = (float*)alloc((size_t)4 * S_ * D_ * 4);

    static bool attr_set = false;
    if (!attr_set) {
        hipFuncSetAttribute((const void*)g1_kernel, hipFuncAttributeMaxDynamicSharedMemorySize, GEMM_LDS);
        hipFuncSetAttribute((const void*)g2_kernel, hipFuncAttributeMaxDynamicSharedMemorySize, GEMM_LDS);
        hipFuncSetAttribute((const void*)g3_kernel, hipFuncAttributeMaxDynamicSharedMemorySize, GEMM_LDS);
        hipFuncSetAttribute((const void*)g4_kernel, hipFuncAttributeMaxDynamicSharedMemorySize, GEMM_LDS);
        attr_set = true;
    }

    transpose_kernel<<<(D_ / 64) * (F_ / 64), 256, 0, stream>>>(w1, w1T, D_, F_);
    transpose_kernel<<<(D_ / 64) * (F_ / 64), 256, 0, stream>>>(w2, w2T, F_, D_);

    const u16* xcur = x0;
    row_kernel<0><<<S_ / 4, 256, 0, stream>>>(x0, nullptr, nullptr, nullptr, xn);
    for (int layer = 0; layer < 2; ++layer) {
        u16* x1 = xa;
        u16* x2 = (layer == 1) ? out : xb;
        g1_kernel<<<128, 256, GEMM_LDS, stream>>>(Hr, Hi, xn, abT);
        g2_kernel<<<256, 256, GEMM_LDS, stream>>>(Cr, Ci, abT, partial);
        row_kernel<4><<<S_ / 4, 256, 0, stream>>>(xcur, partial, nullptr, x1, xn);
        g3_kernel<<<16 * 14, 256, GEMM_LDS, stream>>>(xn, w1T, b1, h2);
        g4_kernel<<<256, 256, GEMM_LDS, stream>>>(h2, w2T, partial);
        row_kernel<4><<<S_ / 4, 256, 0, stream>>>(x1, partial, b2, x2, layer == 1 ? nullptr : xn);
        xcur = x2;
    }
}
```

```cpp
#include <hip/hip_runtime.h>
#include <hip/hip_bf16.h>
#include <stdint.h>

typedef unsigned short u16;
using bf16x8 = __attribute__((ext_vector_type(8))) short;
using f32x4 = __attribute__((ext_vector_type(4))) float;

constexpr int S_ = 2048, D_ = 512, F_ = 1792;

__device__ __forceinline__ float bf2f(u16 u) { return __uint_as_float(((unsigned)u) << 16); }
__device__ __forceinline__ u16 f2bf(float f) { __bf16 h = (__bf16)f; return __builtin_bit_cast(u16, h); }
__device__ __forceinline__ float rbf(float f) { return bf2f(f2bf(f)); }

__global__ __launch_bounds__(256) void prologue_kernel(const u16* __restrict__ x0, const u16* __restrict__ Hr,
                                                       const u16* __restrict__ Hi, const u16* __restrict__ w1,
                                                       const u16* __restrict__ w2, u16* __restrict__ w1T,
                                                       u16* __restrict__ w2T, float* __restrict__ hsum,
                                                       float* __restrict__ w1sum, float2* __restrict__ stats) {
    __shared__ u16 tile[64][66];
    __shared__ float red[4][64];
    int b = blockIdx.x;
    int tid = threadIdx.x, lane = tid & 63, wv = tid >> 6;
    if (b < 448) {
        const u16* src; u16* dst; int R, C; int bb;
        if (b < 224) { src = w1; dst = w1T; R = D_; C = F_; bb = b; } else { src = w2; dst = w2T; R = F_; C = D_; bb = b - 224; }
        int bx = bb % (C / 64), by = bb / (C / 64);
        for (int i = 0; i < 16; ++i) {
            int r = i * 4 + wv;
            tile[r][lane] = src[(long)(by * 64 + r) * C + bx * 64 + lane];
        }
        __syncthreads();
        for (int i = 0; i < 16; ++i) {
            int r = i * 4 + wv;
            dst[(long)(bx * 64 + r) * R + by * 64 + lane] = tile[lane][r];
        }
        if (b < 224 && wv == 0) {
            float s = 0;
            for (int r = 0; r < 64; ++r) s += bf2f(tile[r][lane]);
            w1sum[by * F_ + bx * 64 + lane] = s;
        }
    } else if (b < 476) {
        (void)red;
    } else if (b < 476 + 256) {
        int row = (b - 476) * 4 + wv;
        const u16* src = (row < 512 ? Hr + (long)row * D_ : Hi + (long)(row - 512) * D_) + lane * 8;
        float s = 0;
        for (int i = 0; i < 8; ++i) s += bf2f(src[i]);
        for (int off = 32; off > 0; off >>= 1) s += __shfl_xor(s, off);
        if (lane == 0) hsum[row] = s;
    } else {
        int row = (b - 732) * 4 + wv;
        float v[8];
        const u16* src = x0 + (long)row * D_ + lane * 8;
        float s = 0;
        for (int i = 0; i < 8; ++i) { v[i] = bf2f(src[i]); s += v[i]; }
        for (int off = 4; off > 0; off >>= 1) s += __shfl_xor(s, off);
        float mean = s * (1.f / 64.f);
        float q = 0;
        for (int i = 0; i < 8; ++i) { float d = v[i] - mean; q += d * d; }
        for (int off = 4; off > 0; off >>= 1) q += __shfl_xor(q, off);
        if ((lane & 7) == 0) stats[row * 8 + (lane >> 3)] = make_float2(mean, q);
    }
}

constexpr int G128_LDS = 4 * 32768 + 2048;

template <int MODE>
__global__ __launch_bounds__(256, 1) void lngemm_kernel(const u16* __restrict__ W0, const u16* __restrict__ W1,
                                                        const u16* __restrict__ x, const float* __restrict__ wsum,
                                                        const float2* __restrict__ stats, const u16* __restrict__ bias,
                                                        u16* __restrict__ out) {
    extern __shared__ __attribute__((aligned(16))) char smem[];
    float* smean = (float*)(smem + 4 * 32768);
    float* srden = smean + 128;
    float* swsum = smean + 256;
    float* sbias = smean + 384;
    int tid = threadIdx.x;
    int wave = __builtin_amdgcn_readfirstlane(tid >> 6), lane = tid & 63;
    int wr = wave >> 1, wc = wave & 1;
    int fr = lane & 15, fq = lane >> 4;
    const u16 *A, *B;
    int actrow0, wrow0;
    u16* C; int ldc; float sign = 1.f;
    if (MODE == 0) {
        int tm = blockIdx.x & 7, tn = blockIdx.x >> 3;
        A = (tm < 4 ? W0 : W1) + (long)(tm & 3) * 128 * D_;
        B = x + (long)tn * 128 * D_;
        actrow0 = tn * 128; wrow0 = tm * 128;
        sign = tm < 4 ? 1.f : -1.f;
        C = out + (long)tm * 128 * S_ + tn * 128; ldc = S_;
    } else {
        int tn = blockIdx.x % 14, tm = blockIdx.x / 14;
        A = x + (long)tm * 128 * D_;
        B = W0 + (long)tn * 128 * D_;
        actrow0 = tm * 128; wrow0 = tn * 128;
        C = out + (long)tm * 128 * F_ + tn * 128; ldc = F_;
    }

    f32x4 acc[4][4];
#pragma unroll
    for (int i = 0; i < 4; ++i)
#pragma unroll
        for (int j = 0; j < 4; ++j) acc[i][j] = f32x4{0, 0, 0, 0};
    const u16* ga[4]; const u16* gb[4];
#pragma unroll
    for (int i = 0; i < 4; ++i) {
        int p = (i * 4 + wave) * 1024 + lane * 16;
        int row = p >> 7;
        int c = ((p >> 4) & 7) ^ ((row >> 1) & 7);
        ga[i] = A + (long)row * D_ + c * 8;
        gb[i] = B + (long)row * D_ + c * 8;
    }
    auto stage = [&](int t, int buf) {
#pragma unroll
        for (int i = 0; i < 4; ++i)
            __builtin_amdgcn_global_load_lds((const unsigned*)(ga[i] + t * 64), (unsigned*)(smem + buf * 32768 + (i * 4 + wave) * 1024), 16, 0, 0);
#pragma unroll
        for (int i = 0; i < 4; ++i)
            __builtin_amdgcn_global_load_lds((const unsigned*)(gb[i] + t * 64), (unsigned*)(smem + buf * 32768 + 16384 + (i * 4 + wave) * 1024), 16, 0, 0);
    };
    int aoff[2][4], boff[2][4];
#pragma unroll
    for (int kk = 0; kk < 2; ++kk)
#pragma unroll
        for (int i = 0; i < 4; ++i) {
            int row = wr * 64 + i * 16 + fr;
            aoff[kk][i] = row * 128 + (((kk * 4 + fq) ^ ((row >> 1) & 7)) << 4);
            int rowb = wc * 64 + i * 16 + fr;
            boff[kk][i] = 16384 + rowb * 128 + (((kk * 4 + fq) ^ ((rowb >> 1) & 7)) << 4);
        }
    stage(0, 0); stage(1, 1); stage(2, 2);
    if (tid < 128) {
        const float2* sp = stats + (long)(actrow0 + tid) * 8;
        float mean = 0, m2 = 0;
        {
            float ms[8]; float msum = 0;
#pragma unroll
            for (int c = 0; c < 8; ++c) { float2 v = sp[c]; ms[c] = v.x; m2 += v.y; msum += v.x; }
            mean = msum * 0.125f;
#pragma unroll
            for (int c = 0; c < 8; ++c) { float d = ms[c] - mean; m2 += 64.f * d * d; }
        }
        float var = rbf(m2 * (1.f / D_));
        float den = rbf(sqrtf(rbf(var + 1e-5f)));
        smean[tid] = rbf(mean);
        srden[tid] = 1.f / den;
    } else {
        int w = tid - 128;
        float ws_;
        if (MODE == 0) ws_ = wsum[wrow0 + w];
        else {
            ws_ = 0;
#pragma unroll
            for (int c = 0; c < 8; ++c) ws_ += wsum[c * F_ + wrow0 + w];
            sbias[w] = bf2f(bias[wrow0 + w]);
        }
        swsum[w] = ws_;
    }
    constexpr int NK = 8;
#pragma unroll
    for (int t = 0; t < NK; ++t) {
        int last_issued = (t + 2 < NK - 1) ? (t + 2) : (NK - 1);
        int allowed = (last_issued - t) * 8;
        if (allowed >= 16) asm volatile("s_waitcnt vmcnt(16)" ::: "memory");
        else if (allowed == 8) asm volatile("s_waitcnt vmcnt(8)" ::: "memory");
        else asm volatile("s_waitcnt vmcnt(0)" ::: "memory");
        asm volatile("s_waitcnt lgkmcnt(0)" ::: "memory");
        __builtin_amdgcn_s_barrier();
        if (t + 3 < NK) stage(t + 3, (t + 3) & 3);
        const char* sb = smem + (t & 3) * 32768;
        {
            bf16x8 af[2][4], bfr[2][4];
#pragma unroll
            for (int kk = 0; kk < 2; ++kk) {
#pragma unroll
                for (int i = 0; i < 4; ++i) af[kk][i] = *(const bf16x8*)(sb + aoff[kk][i]);
#pragma unroll
                for (int j = 0; j < 4; ++j) bfr[kk][j] = *(const bf16x8*)(sb + boff[kk][j]);
            }
            __builtin_amdgcn_sched_barrier(0);
#pragma unroll
            for (int kk = 0; kk < 2; ++kk)
#pragma unroll
                for (int i = 0; i < 4; ++i)
#pragma unroll
                    for (int j = 0; j < 4; ++j)
                        acc[i][j] = __builtin_amdgcn_mfma_f32_16x16x32_bf16(bfr[kk][j], af[kk][i], acc[i][j], 0, 0, 0);
            __builtin_amdgcn_sched_barrier(0);
        }
    }
#pragma unroll
    for (int i = 0; i < 4; ++i) {
        int m = wr * 64 + i * 16 + fr;
#pragma unroll
        for (int j = 0; j < 4; ++j) {
            int n = wc * 64 + j * 16 + fq * 4;
            u16 o4[4];
#pragma unroll
            for (int r = 0; r < 4; ++r) {
                if (MODE == 0) {
                    float v = (acc[i][j][r] - smean[n + r] * swsum[m]) * srden[n + r];
                    o4[r] = f2bf(sign * rbf(v));
                } else {
                    float v = (acc[i][j][r] - smean[m] * swsum[n + r]) * srden[m];
                    float h = rbf(rbf(v) + sbias[n + r]);
                    float rr = h > 0.f ? h : 0.f;
                    o4[r] = f2bf(rr * rr);
                }
            }
            uint2 o;
            o.x = o4[0] | ((unsigned)o4[1] << 16);
            o.y = o4[2] | ((unsigned)o4[3] << 16);
            *(uint2*)(C + (long)m * ldc + n) = o;
        }
    }
}

constexpr int G64_LDS = 4 * 32768;

template <int MODE>
__global__ __launch_bounds__(256, 1) void resgemm_kernel(const u16* __restrict__ A0, const u16* __restrict__ A1,
                                                         const u16* __restrict__ Bm, const u16* __restrict__ bias,
                                                         const u16* __restrict__ xold, u16* __restrict__ xnew,
                                                         float2* __restrict__ stats) {
    extern __shared__ __attribute__((aligned(16))) char smem[];
    int tid = threadIdx.x;
    int wave = __builtin_amdgcn_readfirstlane(tid >> 6), lane = tid & 63;
    int fr = lane & 15, fq = lane >> 4;
    int id = (blockIdx.x & 7) * 32 + (blockIdx.x >> 3);
    int tm = id >> 3, tn = id & 7;
    constexpr int NK = MODE == 0 ? 16 : 7;
    constexpr int LDA = MODE == 0 ? S_ : F_;
    const u16 *A, *B;
    if (MODE == 0) {
        A = (wave < 2 ? A0 : A1) + (long)tm * 64 * S_ + (wave & 1) * 1024;
        B = Bm + (long)((wave < 2 ? 0 : 512) + tn * 64) * S_ + (wave & 1) * 1024;
    } else {
        A = A0 + (long)tm * 64 * F_ + wave * 448;
        B = Bm + (long)tn * 64 * F_ + wave * 448;
    }
    char* mybuf = smem + wave * 32768;
    const u16* ga; const u16* gb;
    {
        int row = lane >> 3;
        int c = (lane & 7);
        ga = A + (long)row * LDA; gb = B + (long)row * LDA;
        (void)c;
    }
    auto stage = [&](int t, int buf) {
#pragma unroll
        for (int i = 0; i < 8; ++i) {
            int row = i * 8 + (lane >> 3);
            int c = (lane & 7) ^ ((row >> 1) & 7);
            __builtin_amdgcn_global_load_lds((const unsigned*)(A + (long)row * LDA + t * 64 + c * 8), (unsigned*)(mybuf + buf * 16384 + i * 1024), 16, 0, 0);
        }
#pragma unroll
        for (int i = 0; i < 8; ++i) {
            int row = i * 8 + (lane >> 3);
            int c = (lane & 7) ^ ((row >> 1) & 7);
            __builtin_amdgcn_global_load_lds((const unsigned*)(B + (long)row * LDA + t * 64 + c * 8), (unsigned*)(mybuf + buf * 16384 + 8192 + i * 1024), 16, 0, 0);
        }
    };
    (void)ga; (void)gb;
    f32x4 acc[4][4];
#pragma unroll
    for (int i = 0; i < 4; ++i)
#pragma unroll
        for (int j = 0; j < 4; ++j) acc[i][j] = f32x4{0, 0, 0, 0};
    int off[2][4];
#pragma unroll
    for (int kk = 0; kk < 2; ++kk)
#pragma unroll
        for (int i = 0; i < 4; ++i) {
            int row = i * 16 + fr;
            off[kk][i] = row * 128 + (((kk * 4 + fq) ^ ((row >> 1) & 7)) << 4);
        }
    stage(0, 0);
#pragma unroll
    for (int t = 0; t < NK; ++t) {
        if (t + 1 < NK) { stage(t + 1, (t + 1) & 1); asm volatile("s_waitcnt vmcnt(16)" ::: "memory"); }
        else asm volatile("s_waitcnt vmcnt(0)" ::: "memory");
        const char* sb = mybuf + (t & 1) * 16384;
        {
            bf16x8 af[2][4], bfr[2][4];
#pragma unroll
            for (int kk = 0; kk < 2; ++kk) {
#pragma unroll
                for (int i = 0; i < 4; ++i) af[kk][i] = *(const bf16x8*)(sb + off[kk][i]);
#pragma unroll
                for (int j = 0; j < 4; ++j) bfr[kk][j] = *(const bf16x8*)(sb + 8192 + off[kk][j]);
            }
            __builtin_amdgcn_sched_barrier(0);
#pragma unroll
            for (int kk = 0; kk < 2; ++kk)
#pragma unroll
                for (int i = 0; i < 4; ++i)
#pragma unroll
                    for (int j = 0; j < 4; ++j)
                        acc[i][j] = __builtin_amdgcn_mfma_f32_16x16x32_bf16(bfr[kk][j], af[kk][i], acc[i][j], 0, 0, 0);
            __builtin_amdgcn_sched_barrier(0);
        }
        asm volatile("s_waitcnt lgkmcnt(0)" ::: "memory");
    }
    float* myred = (float*)mybuf;
#pragma unroll
    for (int i = 0; i < 4; ++i)
#pragma unroll
        for (int j = 0; j < 4; ++j)
            *(f32x4*)(myred + (i * 16 + fr) * 68 + j * 16 + fq * 4) = acc[i][j];
    __syncthreads();
    int row = wave * 16 + (lane >> 2);
    int cq = (lane & 3) * 16;
    long grow = (long)tm * 64 + row;
    float v[16];
    const u16* xo = xold + grow * D_ + tn * 64 + cq;
    uint4 xr0 = *(const uint4*)xo, xr1 = *(const uint4*)(xo + 8);
    unsigned xw[8] = {xr0.x, xr0.y, xr0.z, xr0.w, xr1.x, xr1.y, xr1.z, xr1.w};
#pragma unroll
    for (int q = 0; q < 4; ++q) {
        f32x4 p0 = *(const f32x4*)((float*)(smem) + row * 68 + cq + q * 4);
        f32x4 p1 = *(const f32x4*)((float*)(smem + 32768) + row * 68 + cq + q * 4);
        f32x4 p2 = *(const f32x4*)((float*)(smem + 65536) + row * 68 + cq + q * 4);
        f32x4 p3 = *(const f32x4*)((float*)(smem + 98304) + row * 68 + cq + q * 4);
#pragma unroll
        for (int r = 0; r < 4; ++r) {
            float y;
            if (MODE == 0) y = rbf(rbf(p0[r] + p1[r]) + rbf(p2[r] + p3[r]));
            else y = rbf(rbf((p0[r] + p1[r]) + (p2[r] + p3[r])) + bf2f(bias[tn * 64 + cq + q * 4 + r]));
            int e = q * 4 + r;
            float xv = (e & 1) ? __uint_as_float(xw[e >> 1] & 0xffff0000u) : __uint_as_float(xw[e >> 1] << 16);
            v[e] = rbf(xv + y);
        }
    }
    uint4 o0, o1;
    o0.x = f2bf(v[0]) | ((unsigned)f2bf(v[1]) << 16); o0.y = f2bf(v[2]) | ((unsigned)f2bf(v[3]) << 16);
    o0.z = f2bf(v[4]) | ((unsigned)f2bf(v[5]) << 16); o0.w = f2bf(v[6]) | ((unsigned)f2bf(v[7]) << 16);
    o1.x = f2bf(v[8]) | ((unsigned)f2bf(v[9]) << 16); o1.y = f2bf(v[10]) | ((unsigned)f2bf(v[11]) << 16);
    o1.z = f2bf(v[12]) | ((unsigned)f2bf(v[13]) << 16); o1.w = f2bf(v[14]) | ((unsigned)f2bf(v[15]) << 16);
    u16* xo2 = xnew + grow * D_ + tn * 64 + cq;
    *(uint4*)xo2 = o0; *(uint4*)(xo2 + 8) = o1;
    float s = 0;
#pragma unroll
    for (int e = 0; e < 16; ++e) s += v[e];
    s += __shfl_xor(s, 1); s += __shfl_xor(s, 2);
    float mean = s * (1.f / 64.f);
    float q2 = 0;
#pragma unroll
    for (int e = 0; e < 16; ++e) { float d = v[e] - mean; q2 += d * d; }
    q2 += __shfl_xor(q2, 1); q2 += __shfl_xor(q2, 2);
    if ((lane & 3) == 0) stats[grow * 8 + tn] = make_float2(mean, q2);
}

extern "C" void kernel_launch(void* const* d_in, const int* in_sizes, int n_in, void* d_out, int out_size, void* d_ws,
                              size_t ws_size, hipStream_t stream) {
    const u16* x0 = (const u16*)d_in[0];
    const u16* Cr = (const u16*)d_in[1];
    const u16* Ci = (const u16*)d_in[2];
    const u16* Hr = (const u16*)d_in[3];
    const u16* Hi = (const u16*)d_in[4];
    const u16* w1 = (const u16*)d_in[6];
    const u16* b1 = (const u16*)d_in[7];
    const u16* w2 = (const u16*)d_in[8];
    const u16* b2 = (const u16*)d_in[9];
    u16* out = (u16*)d_out;

    char* ws = (char*)d_ws;
    size_t off = 0;
    auto alloc = [&](size_t bytes) { char* p = ws + off; off += (bytes + 255) & ~(size_t)255; return p; };
    u16* w1T = (u16*)alloc((size_t)F_ * D_ * 2);
    u16* w2T = (u16*)alloc((size_t)F_ * D_ * 2);
    u16* xa = (u16*)alloc((size_t)S_ * D_ * 2);
    u16* xb = (u16*)alloc((size_t)S_ * D_ * 2);
    u16* abT = (u16*)alloc((size_t)1024 * S_ * 2);
    u16* h2 = (u16*)alloc((size_t)S_ * F_ * 2);
    float* hsum = (float*)alloc(1024 * 4);
    float* w1sum = (float*)alloc(8 * F_ * 4);
    float2* stats = (float2*)alloc((size_t)S_ * 8 * 8);

    (void)hipFuncSetAttribute((const void*)lngemm_kernel<0>, hipFuncAttributeMaxDynamicSharedMemorySize, G128_LDS);
    (void)hipFuncSetAttribute((const void*)lngemm_kernel<1>, hipFuncAttributeMaxDynamicSharedMemorySize, G128_LDS);
    (void)hipFuncSetAttribute((const void*)resgemm_kernel<0>, hipFuncAttributeMaxDynamicSharedMemorySize, G64_LDS);
    (void)hipFuncSetAttribute((const void*)resgemm_kernel<1>, hipFuncAttributeMaxDynamicSharedMemorySize, G64_LDS);

    prologue_kernel<<<732 + 512, 256, 0, stream>>>(x0, Hr, Hi, w1, w2, w1T, w2T, hsum, w1sum, stats);
    const u16* xcur = x0;
    for (int layer = 0; layer < 2; ++layer) {
        u16* x1 = xa;
        u16* x2 = (layer == 1) ? out : xb;
        lngemm_kernel<0><<<128, 256, G128_LDS, stream>>>(Hr, Hi, xcur, hsum, stats, nullptr, abT);
        resgemm_kernel<0><<<256, 256, G64_LDS, stream>>>(Cr, Ci, abT, nullptr, xcur, x1, stats);
        lngemm_kernel<1><<<224, 256, G128_LDS, stream>>>(w1T, nullptr, x1, w1sum, stats, b1, h2);
        resgemm_kernel<1><<<256, 256, G64_LDS, stream>>>(h2, nullptr, w2T, b2, x1, x2, stats);
        xcur = x2;
    }
}
```
